```python
import torch

def _softmax(x, axis=-1):
    return torch.softmax(x, dim=axis)
def _gelu(x):
    return torch.nn.functional.gelu(x, approximate="tanh")
def _silu(x):
    return torch.nn.functional.silu(x)
def _sigmoid(x):
    return torch.sigmoid(x)
def _layernorm(x, axis=-1, eps=1e-5):
    m = x.mean(dim=axis, keepdim=True)
    v = x.var(dim=axis, keepdim=True, unbiased=False)
    return (x - m) / torch.sqrt(v + eps)
def _scatter_add(tgt, idx, src, axis=0):
    return tgt.clone().index_add_(axis, idx, src)
def _segment_sum(src, seg, n):
    out = torch.zeros((n,) + src.shape[1:], dtype=src.dtype)
    return out.index_add_(0, seg, src)
def _pad(x, pw):
    flat = tuple(v for pair in reversed(pw) for v in pair)
    return torch.nn.functional.pad(x, flat)

kernel_name = 'parallel_sum__linear_kernel_hedgehog__glu_gated_glu__prompt'

def setup_inputs():

    _w = (lambda a: (lambda t: t.to(torch.bfloat16) if t.is_floating_point() else t)(torch.as_tensor(a)))
    def _A_setup():
        B, S, D, H = 1, 2048, 1024, 32
        Dh = D // H
        return {
            "x": _w((lambda *s: torch.randn(*s, dtype=torch.bfloat16) * 0.05)(B, S, D)),
            "wqkv": _w((lambda *s: torch.randn(*s, dtype=torch.bfloat16) * 0.4)(D, 3 * D)),
            "w_phi": _w((lambda *s: torch.randn(*s, dtype=torch.bfloat16) * 0.4)(H, Dh, Dh) * (Dh ** -0.5)),
            "wo": _w((lambda *s: torch.randn(*s, dtype=torch.bfloat16) * 0.1)(D, D)),
        }
    _A_in = _A_setup()

    def _B_setup():
        B, S, D, F = 1, 2048, 1024, 3584
        return {
            "x": _w((lambda *s: torch.randn(*s, dtype=torch.bfloat16) * 0.05)(B, S, D)),
            "w_gate": _w((lambda *s: torch.randn(*s, dtype=torch.bfloat16) * 0.05)(D, F)),
            "w_up": _w((lambda *s: torch.randn(*s, dtype=torch.bfloat16) * 0.05)(D, F)),
            "w_down": _w((lambda *s: torch.randn(*s, dtype=torch.bfloat16) * 0.05)(F, D)),
        }
    _B_in = _B_setup()

    return {
        "A_x": _A_in["x"],
        "A_wqkv": _A_in["wqkv"],
        "A_w_phi": _A_in["w_phi"],
        "A_wo": _A_in["wo"],
        "B_x": _B_in["x"],
        "B_w_gate": _B_in["w_gate"],
        "B_w_up": _B_in["w_up"],
        "B_w_down": _B_in["w_down"],
    }

def reference(A_x, A_wqkv, A_w_phi, A_wo, B_x, B_w_gate, B_w_up, B_w_down):
    def _A_ref(x, wqkv, w_phi, wo):
        B, S, D = x.shape
        H = 32; Dh = D // H
        qkv = (lambda x, *d: x.permute(*d))((x @ wqkv).reshape(B, S, 3, H, Dh), 2, 0, 3, 1, 4)
        q, k, v = qkv[0], qkv[1], qkv[2]
        pq = torch.einsum("bhsd,hdf->bhsf", q, w_phi)
        pk = torch.einsum("bhsd,hdf->bhsf", k, w_phi)
        phi_q = torch.concatenate([_softmax(pq, axis=-1), _softmax(-pq, axis=-1)], axis=-1)
        phi_k = torch.concatenate([_softmax(pk, axis=-1), _softmax(-pk, axis=-1)], axis=-1)
        kv = torch.einsum("bhsf,bhsd->bhfd", phi_k, v)
        z  = torch.einsum("bhsf->bhf", phi_k)
        num = torch.einsum("bhqf,bhfd->bhqd", phi_q, kv)
        den = torch.einsum("bhqf,bhf->bhq", phi_q, z)[..., None] + 1e-6
        o = num / den
        return (lambda x, *d: x.permute(*d))(o, 0, 2, 1, 3).reshape(B, S, D) @ wo

    def _B_ref(x, w_gate, w_up, w_down):
        g = torch.einsum("bsd,df->bsf", x, w_gate)
        u = torch.einsum("bsd,df->bsf", x, w_up)
        h = _sigmoid(g) * u
        return torch.einsum("bsf,fd->bsd", h, w_down)

    x = A_x
    xn = _layernorm(x, axis=-1)
    return x + _A_ref(xn, A_wqkv, A_w_phi, A_wo) + _B_ref(xn, B_w_gate, B_w_up, B_w_down)
```

```cpp
#include <hip/hip_runtime.h>
#include <stdint.h>

typedef unsigned short u16;
typedef short s16x4 __attribute__((ext_vector_type(4)));
typedef short s16x8 __attribute__((ext_vector_type(8)));
typedef __bf16 bf16x8 __attribute__((ext_vector_type(8)));
typedef __bf16 bf16x2 __attribute__((ext_vector_type(2)));
typedef float f32x2 __attribute__((ext_vector_type(2)));
typedef float f32x4 __attribute__((ext_vector_type(4)));
typedef unsigned u32x2 __attribute__((ext_vector_type(2)));
typedef unsigned u32x4 __attribute__((ext_vector_type(4)));
#define LAS __attribute__((address_space(3)))
#define DI __device__ __forceinline__

constexpr int S_ = 2048, D_ = 1024, H_ = 32, F_ = 3584;
constexpr size_t WS_XN = 0;
constexpr size_t WS_AF = 4u << 20;
constexpr size_t AF_BYTES = (size_t)16 * 144 * 8 * 1024;
constexpr size_t WS_PQ = WS_AF + AF_BYTES;
constexpr size_t PQ_BYTES = (size_t)32 * 128 * 2 * 1024;
constexpr size_t WS_KVP = WS_PQ + PQ_BYTES;
constexpr int KVP_STRIDE = 8448;

DI unsigned cvtpk(float lo, float hi) {
  f32x2 v = {lo, hi};
  bf16x2 b = __builtin_convertvector(v, bf16x2);
  return __builtin_bit_cast(unsigned, b);
}
DI float bf_lo(unsigned u) { return __uint_as_float(u << 16); }
DI float bf_hi(unsigned u) { return __uint_as_float(u & 0xffff0000u); }
DI u32x4 make_srd(const void* p, unsigned bytes) {
  uint64_t a = (uint64_t)p;
  u32x4 r;
  r.x = (unsigned)a; r.y = (unsigned)(a >> 32) & 0xffffu; r.z = bytes; r.w = 0x00020000u;
  return r;
}
DI void dma16(unsigned ldsaddr, unsigned voff, u32x4 srd, unsigned soff) {
  asm volatile("s_mov_b32 m0, %0\n\ts_nop 0\n\tbuffer_load_dwordx4 %1, %2, %3 offen lds"
               ::"s"(ldsaddr), "v"(voff), "s"(srd), "s"(soff) : "memory");
}
DI void dma16x2(unsigned ldsaddr, unsigned voff, u32x4 srd, unsigned soff) {
  asm volatile("s_mov_b32 m0, %0\n\ts_nop 0\n\tbuffer_load_dwordx4 %1, %2, %3 offen lds\n\t"
               "buffer_load_dwordx4 %1, %2, %3 offen offset:1024 lds"
               ::"s"(ldsaddr), "v"(voff), "s"(srd), "s"(soff) : "memory");
}
DI bf16x8 tr2(LAS const char* p0, LAS const char* p1) {
  s16x4 a = __builtin_amdgcn_ds_read_tr16_b64_v4i16((LAS s16x4*)p0);
  s16x4 b = __builtin_amdgcn_ds_read_tr16_b64_v4i16((LAS s16x4*)p1);
  return __builtin_bit_cast(bf16x8, __builtin_shufflevector(a, b, 0, 1, 2, 3, 4, 5, 6, 7));
}
DI f32x4 mfma16(bf16x8 a, bf16x8 b, f32x4 c) { return __builtin_amdgcn_mfma_f32_16x16x32_bf16(a, b, c, 0, 0, 0); }
DI void mfma16a(f32x4& c, bf16x8 a, bf16x8 b) {
  asm volatile("v_mfma_f32_16x16x32_bf16 %0, %1, %2, %0" : "+v"(c) : "v"(a), "v"(b));
}
DI bf16x8 pack8(f32x4 a, f32x4 b) {
  u32x4 w;
  w.x = cvtpk(a[0], a[1]); w.y = cvtpk(a[2], a[3]); w.z = cvtpk(b[0], b[1]); w.w = cvtpk(b[2], b[3]);
  return __builtin_bit_cast(bf16x8, w);
}
#define SCHED() __builtin_amdgcn_sched_barrier(0)
#define BAR() asm volatile("s_barrier" ::: "memory")

__global__ __launch_bounds__(512) void k_ln(const u16* __restrict__ x, char* __restrict__ ws) {
  __shared__ float red[2][8][16];
  const int tid = threadIdx.x, w = tid >> 6, lane = tid & 63, fr = lane & 15, fq = lane >> 4;
  const int rb = blockIdx.x;
  const u16* xr = x + (size_t)(rb * 16 + fr) * D_ + w * 128 + fq * 8;
  u32x4 v[4];
#pragma unroll
  for (int i = 0; i < 4; ++i) v[i] = *(const u32x4*)(xr + i * 32);
  float f[32];
#pragma unroll
  for (int i = 0; i < 4; ++i)
#pragma unroll
    for (int j = 0; j < 4; ++j) { f[i * 8 + 2 * j] = bf_lo(v[i][j]); f[i * 8 + 2 * j + 1] = bf_hi(v[i][j]); }
  float s = 0.f;
#pragma unroll
  for (int i = 0; i < 32; ++i) s += f[i];
  s += __shfl_xor(s, 16); s += __shfl_xor(s, 32);
  if (fq == 0) red[0][w][fr] = s;
  __syncthreads();
  float mean = 0.f;
#pragma unroll
  for (int i = 0; i < 8; ++i) mean += red[0][i][fr];
  mean *= (1.0f / D_);
  float q = 0.f;
#pragma unroll
  for (int i = 0; i < 32; ++i) { f[i] -= mean; q += f[i] * f[i]; }
  q += __shfl_xor(q, 16); q += __shfl_xor(q, 32);
  if (fq == 0) red[1][w][fr] = q;
  __syncthreads();
  float var = 0.f;
#pragma unroll
  for (int i = 0; i < 8; ++i) var += red[1][i][fr];
  const float rstd = 1.0f / sqrtf(var * (1.0f / D_) + 1e-5f);
  char* dst = ws + WS_XN + ((size_t)rb * 32 + w * 4) * 1024 + lane * 16;
#pragma unroll
  for (int i = 0; i < 4; ++i) {
    u32x4 o;
#pragma unroll
    for (int j = 0; j < 4; ++j) o[j] = cvtpk(f[i * 8 + 2 * j] * rstd, f[i * 8 + 2 * j + 1] * rstd);
    *(u32x4*)(dst + i * 1024) = o;
  }
}

constexpr int L1_A = 0;
constexpr int L1_B = 65536;
constexpr int L1_WPHI = 147456;
constexpr int L1_TOTAL = 151552;
#define MBAR 32

DI float xor16_max(float v) { return fmaxf(v, __shfl_xor(v, 16)); }
DI float xor32_max(float v) { return fmaxf(v, __shfl_xor(v, 32)); }

DI void dual_softmax(f32x4 p0, f32x4 p1, bf16x8& pos, bf16x8& neg) {
  float mx = fmaxf(fmaxf(fmaxf(p0[0], p0[1]), fmaxf(p0[2], p0[3])), fmaxf(fmaxf(p1[0], p1[1]), fmaxf(p1[2], p1[3])));
  float mn = fminf(fminf(fminf(p0[0], p0[1]), fminf(p0[2], p0[3])), fminf(fminf(p1[0], p1[1]), fminf(p1[2], p1[3])));
  mx = fmaxf(mx, __shfl_xor(mx, 16)); mx = fmaxf(mx, __shfl_xor(mx, 32));
  mn = fminf(mn, __shfl_xor(mn, 16)); mn = fminf(mn, __shfl_xor(mn, 32));
  const float L2E = 1.4426950408889634f;
  f32x4 a0, a1, b0, b1;
  float sp = 0.f, sn = 0.f;
#pragma unroll
  for (int r = 0; r < 4; ++r) {
    a0[r] = __builtin_amdgcn_exp2f((p0[r] - mx) * L2E); a1[r] = __builtin_amdgcn_exp2f((p1[r] - mx) * L2E);
    b0[r] = __builtin_amdgcn_exp2f((mn - p0[r]) * L2E); b1[r] = __builtin_amdgcn_exp2f((mn - p1[r]) * L2E);
    sp += a0[r] + a1[r]; sn += b0[r] + b1[r];
  }
  sp += __shfl_xor(sp, 16); sp += __shfl_xor(sp, 32);
  sn += __shfl_xor(sn, 16); sn += __shfl_xor(sn, 32);
  const float rp = __builtin_amdgcn_rcpf(sp), rn = __builtin_amdgcn_rcpf(sn);
  pos = pack8(a0 * rp, a1 * rp);
  neg = pack8(b0 * rn, b1 * rn);
}
DI f32x4 glu4(f32x4 g, f32x4 u) {
  f32x4 h;
#pragma unroll
  for (int r = 0; r < 4; ++r) {
    const float e = __builtin_amdgcn_exp2f(g[r] * -1.4426950408889634f);
    h[r] = u[r] * __builtin_amdgcn_rcpf(1.0f + e);
  }
  return h;
}

template <int TYPE>
DI void epilogue1(LAS char* lds, f32x4 (&acc)[10][4], char* __restrict__ ws, const int p, const int j, const int wave);
template <int TYPE  >
DI void gemm1_body(LAS char* lds, const u16* __restrict__ wqkv, const u16* __restrict__ wgate, const u16* __restrict__ wup,
                   const u16* __restrict__ wphi, char* __restrict__ ws, const int p, const int j) {
  const int tid = threadIdx.x, lane = tid & 63;
  const int wave = __builtin_amdgcn_readfirstlane(tid >> 6);
  const int G = wave >> 2, iw = wave & 3, wri = iw >> 1, wc = iw & 1;
  const int fr = lane & 15, fq = lane >> 4;
  const unsigned ldsb = (unsigned)(size_t)lds;

  const u32x4 srdX = make_srd(ws + WS_XN, 4u << 20);
  const u32x4 srdQ = make_srd(wqkv, D_ * 3 * D_ * 2);
  const u32x4 srdG = make_srd(wgate, D_ * F_ * 2);
  const u32x4 srdU = make_srd(wup, D_ * F_ * 2);
  const unsigned vA = lane * 16 + (unsigned)(16 * p + 8 * (1 - G) + 2 * iw) * 32768u;
  const int kl = 32 * G + 8 * iw + (lane >> 3);
  const int cpair = ((lane & 7) >> 1) ^ (((lane >> 4) & 1) | ((iw & 1) << 1));
  const int colo = 16 * cpair + 8 * (lane & 1);
  const unsigned vQ = (unsigned)kl * 6144u + (unsigned)((TYPE == 0 ? 1024 : 0) + 64 * j + colo) * 2u;
  const int fbase = TYPE == 0 ? 2048 + 96 * j : 128 * j;
  const unsigned vG = (unsigned)kl * 7168u + (unsigned)(fbase + colo) * 2u;
  const int kl4 = 32 * G + 16 * (iw & 1) + (lane >> 2);
  const int c4 = ((lane & 3) >> 1) ^ ((lane >> 5) & 1);
  const unsigned v4 = (unsigned)kl4 * 7168u + (unsigned)(fbase + 64 + 16 * c4 + 8 * (lane & 1)) * 2u;
  const u32x4 srd4 = (iw < 2) ? srdG : srdU;

  auto issueA = [&](int t) {
    const unsigned base = ldsb + L1_A + (t & 1) * 32768 + (1 - G) * 16384 + iw * 4096;
    dma16x2(base, vA, srdX, t * 2048);
    dma16x2(base + 2048, vA, srdX, t * 2048 + 32768);
  };
  auto issueB = [&](int t) {
    const unsigned base = ldsb + L1_B + ((t & 1) * 2 + G) * 20480 + iw * 1024;
    const unsigned sq = t * (64 * 6144), sg = t * (64 * 7168);
    if (TYPE == 0) {
      dma16(base, vQ, srdQ, sq);
      dma16(base + 4096, vQ, srdQ, sq + 2048);
      dma16(base + 8192, vG, srdG, sg);
      dma16(base + 12288, vG, srdU, sg);
      dma16(base + 16384, v4, srd4, sg);
    } else {
      dma16(base, vQ, srdQ, sq);
      dma16(base + 4096, vG, srdG, sg);
      dma16(base + 8192, vG, srdU, sg);
      dma16(base + 12288, vG, srdG, sg + 128);
      dma16(base + 16384, vG, srdU, sg + 128);
    }
  };
  const int q4 = (lane & 15) >> 2, p4 = lane & 3;
  const int swz = ((q4 >> 1) & 1) | ((fq & 1) << 1);
  LAS const char* pA = lds + L1_A + G * 16384 + wri * 8192 + lane * 16;
  LAS const char* pB0l = lds + L1_B + 128 * (8 * fq + q4) + 32 * ((2 * wc) ^ swz) + 8 * p4;
  LAS const char* pB1l = lds + L1_B + 128 * (8 * fq + q4) + 32 * ((2 * wc + 1) ^ swz) + 8 * p4;
  LAS const char* pB4l = lds + L1_B + 16384 + 64 * (8 * fq + q4) + 32 * (wc ^ (fq & 1)) + 8 * p4;
  LAS const char* pB0h = pB0l + 40960;
  LAS const char* pB1h = pB1l + 40960;
  LAS const char* pB4h = pB4l + 40960;
  asm volatile("" : "+v"(pB0l), "+v"(pB1l), "+v"(pB0h), "+v"(pB1h));
  if (TYPE == 0) asm volatile("" : "+v"(pB4l), "+v"(pB4h));

  f32x4 acc[10][4];
#pragma unroll
  for (int n = 0; n < 10; ++n)
#pragma unroll
    for (int i = 0; i < 4; ++i) acc[n][i] = (f32x4){0.f, 0.f, 0.f, 0.f};
  bf16x8 Af[4], Bf[10];

  if (wave < 4) {
    const u32x4 srdP = make_srd(wphi, H_ * 32 * 32 * 2);
    dma16(ldsb + L1_WPHI + wave * 1024, lane * 16, srdP, (unsigned)(2 * j) * 2048u + wave * 1024);
  }
  if (G == 0) { issueB(0); issueA(0); issueB(1); } else { issueA(0); issueB(0); issueA(1); }
  asm volatile("s_waitcnt vmcnt(9)" ::: "memory");
  BAR();
  if (G == 1) BAR();

  auto Lphase = [&](auto bufc, auto ksc, int t, auto modec) {
    constexpr int buf = decltype(bufc)::value, ks = decltype(ksc)::value, mode = decltype(modec)::value;
    constexpr int bo = ks * 20480;
    LAS const char* pB0 = buf ? pB0h : pB0l;
    LAS const char* pB1 = buf ? pB1h : pB1l;
    LAS const char* pB4 = buf ? pB4h : pB4l;
#pragma unroll
    for (int i = 0; i < 4; ++i) Af[i] = *(LAS const bf16x8*)(pA + buf * 32768 + i * 2048 + ks * 1024);
#pragma unroll
    for (int b = 0; b < 4; ++b) {
      Bf[2 * b] = tr2(pB0 + bo + b * 4096, pB0 + bo + b * 4096 + 512);
      Bf[2 * b + 1] = tr2(pB1 + bo + b * 4096, pB1 + bo + b * 4096 + 512);
    }
    if (TYPE == 0) {
      Bf[8] = tr2(pB4 + bo, pB4 + bo + 256);
      Bf[9] = tr2(pB4 + bo + 2048, pB4 + bo + 2048 + 256);
    } else {
      Bf[8] = tr2(pB0 + bo + 16384, pB0 + bo + 16384 + 512);
      Bf[9] = tr2(pB1 + bo + 16384, pB1 + bo + 16384 + 512);
    }
    if (mode == 0) {
      if ((G ^ ks) == 0) issueA(t + 1 + ks); else issueB(t + 1 + ks);
      __builtin_amdgcn_s_waitcnt(0xC07F);
      asm volatile("s_waitcnt vmcnt(9)\n\ts_barrier" ::: "memory");
    } else {
      __builtin_amdgcn_s_waitcnt(0xC07F);
      asm volatile("s_waitcnt vmcnt(0)\n\ts_barrier" ::: "memory");
    }
    SCHED();
  };
  auto Mphase = [&]() {
    __builtin_amdgcn_s_setprio(1);
#pragma unroll
    for (int n = 0; n < 10; ++n) {
#pragma unroll
      for (int i = 0; i < 4; ++i) {
        mfma16a(acc[n][i], Bf[n], Af[i]);
        if (n * 4 + i + 1 == MBAR) { BAR(); }
      }
    }
    __builtin_amdgcn_s_setprio(0);
    SCHED();
  };
  using I0 = std::integral_constant<int, 0>;
  using I1 = std::integral_constant<int, 1>;
  for (int t = 0; t < 14; t += 2) {
    Lphase(I0{}, I0{}, t, I0{}); Mphase();
    Lphase(I0{}, I1{}, t, I0{}); Mphase();
    Lphase(I1{}, I0{}, t + 1, I0{}); Mphase();
    Lphase(I1{}, I1{}, t + 1, I0{}); Mphase();
  }
  Lphase(I0{}, I0{}, 14, I0{}); Mphase();
  Lphase(I0{}, I1{}, 14, I1{}); Mphase();
  Lphase(I1{}, I0{}, 15, I1{}); Mphase();
  Lphase(I1{}, I1{}, 15, I1{}); Mphase();
  asm volatile("s_nop 15" ::: "memory");
  if (G == 0) BAR();
  BAR();

  epilogue1<TYPE>(lds, acc, ws, p, j, wave);
}
template <int TYPE>
DI void epilogue1(LAS char* lds, f32x4 (&acc)[10][4], char* __restrict__ ws, const int p, const int j, const int wave) {
  int tz = threadIdx.x;
  asm volatile("" : "+v"(tz));
  const int lane = tz & 63;
  const int G = wave >> 2, iw = wave & 3, wri = iw >> 1, wc = iw & 1;
  const int fr = lane & 15, fq = lane >> 4;
  const int q4 = (lane & 15) >> 2, p4 = lane & 3;
  const int rbw = 16 * p + 8 * G + 4 * wri;
  char* afb = ws + WS_AF + ((size_t)(rbw >> 3) * 144 * 8 + (rbw & 7)) * 1024 + lane * 16;
  if (TYPE == 0) {
    const int ksb = 64 + 3 * j;
#pragma unroll
    for (int i = 0; i < 4; ++i) {
      const bf16x8 h0 = pack8(glu4(acc[4][i], acc[6][i]), glu4(acc[5][i], acc[7][i]));
      *(bf16x8*)(afb + (size_t)(ksb + wc) * 8192 + i * 1024) = h0;
      const f32x4 h1 = glu4(acc[8][i], acc[9][i]);
      u32x2 w; w.x = cvtpk(h1[0], h1[1]); w.y = cvtpk(h1[2], h1[3]);
      *(u32x2*)(afb + (size_t)(ksb + 2) * 8192 + i * 1024 + wc * 8) = w;
    }
  } else {
    const int ksb = 4 * j;
#pragma unroll
    for (int i = 0; i < 4; ++i) {
      const bf16x8 h0 = pack8(glu4(acc[2][i], acc[4][i]), glu4(acc[3][i], acc[5][i]));
      *(bf16x8*)(afb + (size_t)(ksb + wc) * 8192 + i * 1024) = h0;
      const bf16x8 h1 = pack8(glu4(acc[6][i], acc[8][i]), glu4(acc[7][i], acc[9][i]));
      *(bf16x8*)(afb + (size_t)(ksb + 2 + wc) * 8192 + i * 1024) = h1;
    }
  }
  bf16x8 Wp[2];
  {
    LAS const char* pw = lds + L1_WPHI + wc * 2048 + 64 * (4 * fq + q4) + 8 * p4;
    Wp[0] = tr2(pw, pw + 1024);
    Wp[1] = tr2(pw + 32, pw + 1024 + 32);
  }
  const f32x4 z4 = {0.f, 0.f, 0.f, 0.f};
  if (TYPE == 1) {
    char* pq = ws + WS_PQ + ((size_t)(2 * j + wc) * 128 + rbw) * 2048 + lane * 16;
#pragma unroll
    for (int i = 0; i < 4; ++i) {
      const bf16x8 qf = pack8(acc[0][i], acc[1][i]);
      const f32x4 p0 = mfma16(Wp[0], qf, z4), p1 = mfma16(Wp[1], qf, z4);
      bf16x8 pos, neg;
      dual_softmax(p0, p1, pos, neg);
      *(bf16x8*)(pq + i * 2048) = pos;
      *(bf16x8*)(pq + i * 2048 + 1024) = neg;
    }
  } else {
    constexpr int PH = 0, VV = 65536;
    const int fm = (((fr >> 1) & 3) << 2) | ((fr & 1) << 1) | ((fr >> 3) & 1);
    const int fv = (((fr >> 2) & 1) << 2) | (((fr >> 1) & 1) << 1) | ((fr >> 3) & 1);
#pragma unroll
    for (int i = 0; i < 4; ++i) {
      const int m = 128 * G + 64 * wri + 16 * i + fr;
      const bf16x8 kf = pack8(acc[0][i], acc[1][i]);
      const f32x4 p0 = mfma16(Wp[0], kf, z4), p1 = mfma16(Wp[1], kf, z4);
      bf16x8 pos, neg;
      dual_softmax(p0, p1, pos, neg);
      const u32x4 pw = __builtin_bit_cast(u32x4, pos), nw = __builtin_bit_cast(u32x4, neg);
      LAS char* ph = lds + PH + wc * 32768 + m * 128;
      *(LAS u32x2*)(ph + 8 * ((0 + fq) ^ fm)) = (u32x2){pw.x, pw.y};
      *(LAS u32x2*)(ph + 8 * ((4 + fq) ^ fm)) = (u32x2){pw.z, pw.w};
      *(LAS u32x2*)(ph + 8 * ((8 + fq) ^ fm)) = (u32x2){nw.x, nw.y};
      *(LAS u32x2*)(ph + 8 * ((12 + fq) ^ fm)) = (u32x2){nw.z, nw.w};
      LAS char* pv = lds + VV + wc * 16384 + m * 64;
      *(LAS u32x2*)(pv + 8 * ((0 + fq) ^ fv)) = (u32x2){cvtpk(acc[2][i][0], acc[2][i][1]), cvtpk(acc[2][i][2], acc[2][i][3])};
      *(LAS u32x2*)(pv + 8 * ((4 + fq) ^ fv)) = (u32x2){cvtpk(acc[3][i][0], acc[3][i][1]), cvtpk(acc[3][i][2], acc[3][i][3])};
    }
    asm volatile("s_waitcnt lgkmcnt(0)\n\ts_barrier" ::: "memory");
    const int hh = wave >> 2, jf4 = wave & 3;
    const int mr = 4 * fq + q4;
    const int fm2 = (((mr >> 1) & 3) << 2) | ((mr & 1) << 1) | ((mr >> 3) & 1);
    const int fv2 = (((mr >> 2) & 1) << 2) | (((mr >> 1) & 1) << 1) | ((mr >> 3) & 1);
    LAS const char* rph = lds + PH + hh * 32768 + mr * 128 + 8 * ((4 * jf4 + p4) ^ fm2);
    LAS const char* rv0 = lds + VV + hh * 16384 + mr * 64 + 8 * ((0 + p4) ^ fv2);
    LAS const char* rv1 = lds + VV + hh * 16384 + mr * 64 + 8 * ((4 + p4) ^ fv2);
    const u32x4 onesw = {0x3f803f80u, 0x3f803f80u, 0x3f803f80u, 0x3f803f80u};
    const bf16x8 ones = __builtin_bit_cast(bf16x8, onesw);
    f32x4 d0 = z4, d1 = z4, dz = z4;
#pragma unroll
    for (int s = 0; s < 8; ++s) {
      const bf16x8 a = tr2(rph + s * 4096, rph + s * 4096 + 2048);
      const bf16x8 b0 = tr2(rv0 + s * 2048, rv0 + s * 2048 + 1024);
      const bf16x8 b1 = tr2(rv1 + s * 2048, rv1 + s * 2048 + 1024);
      d0 = mfma16(a, b0, d0); d1 = mfma16(a, b1, d1); dz = mfma16(a, ones, dz);
    }
    char* kv = ws + WS_KVP + (size_t)(p * 32 + 2 * j + hh) * KVP_STRIDE;
    *(f32x4*)(kv + (0 * 4 + jf4) * 1024 + lane * 16) = d0;
    *(f32x4*)(kv + (1 * 4 + jf4) * 1024 + lane * 16) = d1;
    if (fr == 0) *(f32x4*)(kv + 8192 + jf4 * 64 + fq * 16) = dz;
  }
}

__global__ __launch_bounds__(512, 2) void k_gemm1(const u16* __restrict__ wqkv, const u16* __restrict__ wgate,
                                                  const u16* __restrict__ wup, const u16* __restrict__ wphi,
                                                  char* __restrict__ ws) {
  extern __shared__ __attribute__((aligned(16))) char smem[];
  LAS char* lds = (LAS char*)smem;
  const int bid = blockIdx.x, xcd = bid & 7, idx = bid >> 3;
  const int p = idx & 7, c = 4 * xcd + (idx >> 3);
  if (c & 1) gemm1_body<1>(lds, wqkv, wgate, wup, wphi, ws, p, c >> 1);
  else gemm1_body<0>(lds, wqkv, wgate, wup, wphi, ws, p, c >> 1);
}

__global__ __launch_bounds__(512) void k_o(char* __restrict__ ws) {
  __shared__ __attribute__((aligned(16))) char kvs[6 * 1024];
  const int tid = threadIdx.x, wave = tid >> 6, lane = tid & 63, fq = lane >> 4;
  const int h = blockIdx.x & 31, p = blockIdx.x >> 5;
  const char* kvp = ws + WS_KVP + (size_t)h * KVP_STRIDE;
  const int rb = 16 * p + 2 * wave;
  const char* pq = ws + WS_PQ + ((size_t)h * 128 + rb) * 2048 + lane * 16;
  bf16x8 ph[2][2];
#pragma unroll
  for (int i = 0; i < 2; ++i)
#pragma unroll
    for (int hf = 0; hf < 2; ++hf) ph[i][hf] = *(const bf16x8*)(pq + i * 2048 + hf * 1024);
  {
    f32x4 s = {0.f, 0.f, 0.f, 0.f};
#pragma unroll
    for (int pp = 0; pp < 8; ++pp) s += *(const f32x4*)(kvp + (size_t)pp * 32 * KVP_STRIDE + wave * 1024 + lane * 16);
    const int jd = wave >> 2, jf4 = wave & 3;
    *(u32x2*)(kvs + (jd * 2 + (jf4 >> 1)) * 1024 + lane * 16 + (jf4 & 1) * 8) = (u32x2){cvtpk(s[0], s[1]), cvtpk(s[2], s[3])};
    if (wave < 4) {
      f32x4 z = {0.f, 0.f, 0.f, 0.f};
#pragma unroll
      for (int pp = 0; pp < 8; ++pp) z += *(const f32x4*)(kvp + (size_t)pp * 32 * KVP_STRIDE + 8192 + wave * 64 + fq * 16);
      *(u32x2*)(kvs + (4 + (wave >> 1)) * 1024 + lane * 16 + (wave & 1) * 8) = (u32x2){cvtpk(z[0], z[1]), cvtpk(z[2], z[3])};
    }
  }
  __syncthreads();
  bf16x8 kf[3][2];
#pragma unroll
  for (int jd = 0; jd < 3; ++jd)
#pragma unroll
    for (int hf = 0; hf < 2; ++hf) kf[jd][hf] = *(const bf16x8*)(kvs + (jd * 2 + hf) * 1024 + lane * 16);
  const f32x4 z4 = {0.f, 0.f, 0.f, 0.f};
#pragma unroll
  for (int i = 0; i < 2; ++i) {
    f32x4 o0 = mfma16(kf[0][1], ph[i][1], mfma16(kf[0][0], ph[i][0], z4));
    f32x4 o1 = mfma16(kf[1][1], ph[i][1], mfma16(kf[1][0], ph[i][0], z4));
    f32x4 dn = mfma16(kf[2][1], ph[i][1], mfma16(kf[2][0], ph[i][0], z4));
    const float r = 1.0f / (dn[0] + 1e-6f);
    const int rbi = rb + i;
    char* dst = ws + WS_AF + (((size_t)(rbi >> 3) * 144 + 112 + h) * 8 + (rbi & 7)) * 1024 + lane * 16;
    *(bf16x8*)dst = pack8(o0 * r, o1 * r);
  }
}

constexpr int D2 = 6;
constexpr int ST2 = 24576;
constexpr int L2_TOTAL = D2 * ST2;
constexpr int NT2 = 72;

__global__ __launch_bounds__(512, 2) void k_gemm2(const u16* __restrict__ x, const u16* __restrict__ wo,
                                                  const u16* __restrict__ wdown, const char* __restrict__ ws,
                                                  u16* __restrict__ out) {
  extern __shared__ __attribute__((aligned(16))) char smem[];
  LAS char* lds = (LAS char*)smem;
  const unsigned ldsb = (unsigned)(size_t)lds;
  const int tid = threadIdx.x, lane = tid & 63;
  const int wave = __builtin_amdgcn_readfirstlane(tid >> 6);
  const int bid = blockIdx.x, xcd = bid & 7, idx = bid >> 3;
  const int rt = 4 * (xcd >> 1) + (idx & 3), ct = 8 * (xcd & 1) + (idx >> 2);
  const int wk = wave >> 2, wm = (wave >> 1) & 1, wn = wave & 1;
  const int fr = lane & 15, fq = lane >> 4, q4 = fr >> 2, p4 = lane & 3;

  const u32x4 srdA = make_srd(ws + WS_AF, (unsigned)AF_BYTES);
  const u32x4 srdD = make_srd(wdown, F_ * D_ * 2);
  const u32x4 srdO = make_srd(wo, D_ * D_ * 2);
  const unsigned vA = (unsigned)rt * (144u * 8192u) + wave * 2048 + lane * 16;
  const int kl = 8 * wave + (lane >> 3);
  const int cp = ((lane & 7) >> 1) ^ ((kl >> 1) & 3);
  const unsigned vB = (unsigned)kl * 2048u + (unsigned)(64 * ct + 16 * cp + 8 * (lane & 1)) * 2u;
  auto issue = [&](int t, int slot) {
    const unsigned base = ldsb + slot * ST2;
    dma16x2(base + wave * 2048, vA, srdA, t * 16384);
    if (t < 56) dma16(base + 16384 + wave * 1024, vB, srdD, t * 131072);
    else dma16(base + 16384 + wave * 1024, vB, srdO, (t - 56) * 131072);
  };
  LAS const char* pA = lds + wk * 8192 + wm * 4096 + lane * 16;
  const int kr = 4 * fq + q4;
  const int sw = (kr >> 1) & 3;
  LAS const char* pB0 = lds + 16384 + (32 * wk + kr) * 128 + 32 * ((2 * wn) ^ sw) + 8 * p4;
  LAS const char* pB1 = lds + 16384 + (32 * wk + kr) * 128 + 32 * ((2 * wn + 1) ^ sw) + 8 * p4;

  f32x4 acc[2][4];
#pragma unroll
  for (int n = 0; n < 2; ++n)
#pragma unroll
    for (int i = 0; i < 4; ++i) acc[n][i] = (f32x4){0.f, 0.f, 0.f, 0.f};
  bf16x8 Aa[4], Ba[2], Ab[4], Bb[2];
  auto rd = [&](bf16x8 (&A)[4], bf16x8 (&B)[2], int slot) {
    const int so = slot * ST2;
#pragma unroll
    for (int i = 0; i < 4; ++i) A[i] = *(LAS const bf16x8*)(pA + so + i * 1024);
    B[0] = tr2(pB0 + so, pB0 + so + 2048);
    B[1] = tr2(pB1 + so, pB1 + so + 2048);
  };
  auto mm = [&](bf16x8 (&A)[4], bf16x8 (&B)[2]) {
#pragma unroll
    for (int n = 0; n < 2; ++n)
#pragma unroll
      for (int i = 0; i < 4; ++i) acc[n][i] = mfma16(B[n], A[i], acc[n][i]);
  };
#pragma unroll
  for (int t = 0; t < D2 - 1; ++t) issue(t, t);
  asm volatile("s_waitcnt vmcnt(%0)\n\ts_barrier" ::"n"(3 * (D2 - 2)) : "memory");
  rd(Aa, Ba, 0);
  for (int t0 = 0; t0 < NT2; t0 += 6) {
#pragma unroll
    for (int u = 0; u < 6; ++u) {
      const int t = t0 + u;
      if (t + D2 - 2 < NT2) asm volatile("s_waitcnt vmcnt(%0)\n\ts_barrier" ::"n"(3 * (D2 - 3)) : "memory");
      else asm volatile("s_waitcnt vmcnt(0)\n\ts_barrier" ::: "memory");
      if (t + D2 - 1 < NT2) issue(t + D2 - 1, (u + D2 - 1) % D2);
      if (u & 1) { if (t + 1 < NT2) rd(Aa, Ba, (u + 1) % D2); mm(Ab, Bb); }
      else { if (t + 1 < NT2) rd(Ab, Bb, (u + 1) % D2); mm(Aa, Ba); }
    }
  }
  asm volatile("s_waitcnt vmcnt(0) lgkmcnt(0)\n\ts_barrier" ::: "memory");
  {
    LAS char* r = lds + wk * (128 * 272) + (64 * wm + fr) * 272 + (32 * wn + 4 * fq) * 4;
#pragma unroll
    for (int n = 0; n < 2; ++n)
#pragma unroll
      for (int i = 0; i < 4; ++i) *(LAS f32x4*)(r + i * 16 * 272 + n * 64) = acc[n][i];
  }
  asm volatile("s_waitcnt lgkmcnt(0)\n\ts_barrier" ::: "memory");
  {
    const int row = tid >> 2, c0 = (tid & 3) * 16;
    LAS const char* r = lds + row * 272 + c0 * 4;
    const size_t go = (size_t)(128 * rt + row) * D_ + 64 * ct + c0;
    const u32x4 x0 = *(const u32x4*)(x + go), x1 = *(const u32x4*)(x + go + 8);
    u32x4 o0, o1;
#pragma unroll
    for (int e = 0; e < 4; ++e) {
      const f32x4 a = *(LAS const f32x4*)(r + e * 16) + *(LAS const f32x4*)(r + 128 * 272 + e * 16);
      const unsigned xa = e < 2 ? x0[2 * e] : x1[2 * e - 4], xb = e < 2 ? x0[2 * e + 1] : x1[2 * e - 3];
      const unsigned wa = cvtpk(a[0] + bf_lo(xa), a[1] + bf_hi(xa)), wb = cvtpk(a[2] + bf_lo(xb), a[3] + bf_hi(xb));
      if (e < 2) { o0[2 * e] = wa; o0[2 * e + 1] = wb; } else { o1[2 * e - 4] = wa; o1[2 * e - 3] = wb; }
    }
    *(u32x4*)(out + go) = o0;
    *(u32x4*)(out + go + 8) = o1;
  }
}

extern "C" void kernel_launch(void* const* d_in, const int* in_sizes, int n_in, void* d_out, int out_size, void* d_ws,
                              size_t ws_size, hipStream_t stream) {
  const u16* x = (const u16*)d_in[0];
  const u16* wqkv = (const u16*)d_in[1];
  const u16* wphi = (const u16*)d_in[2];
  const u16* wo = (const u16*)d_in[3];
  const u16* wgate = (const u16*)d_in[5];
  const u16* wup = (const u16*)d_in[6];
  const u16* wdown = (const u16*)d_in[7];
  char* ws = (char*)d_ws;
  (void)hipFuncSetAttribute((const void*)k_gemm1, hipFuncAttributeMaxDynamicSharedMemorySize, L1_TOTAL);
  (void)hipFuncSetAttribute((const void*)k_gemm2, hipFuncAttributeMaxDynamicSharedMemorySize, L2_TOTAL);
  k_ln<<<128, 512, 0, stream>>>(x, ws);
  k_gemm1<<<256, 512, L1_TOTAL, stream>>>(wqkv, wgate, wup, wphi, ws);
  k_o<<<256, 512, 0, stream>>>(ws);
  k_gemm2<<<256, 512, L2_TOTAL, stream>>>(x, wo, wdown, ws, (u16*)d_out);
}
```

```cpp
#include <hip/hip_runtime.h>
#include <stdint.h>
#include <type_traits>

typedef unsigned short u16;
typedef short s16x4 __attribute__((ext_vector_type(4)));
typedef __bf16 bf16x8 __attribute__((ext_vector_type(8)));
typedef __bf16 bf16x2 __attribute__((ext_vector_type(2)));
typedef float f32x2 __attribute__((ext_vector_type(2)));
typedef float f32x4 __attribute__((ext_vector_type(4)));
typedef unsigned u32x2 __attribute__((ext_vector_type(2)));
typedef unsigned u32x4 __attribute__((ext_vector_type(4)));
typedef __attribute__((address_space(1))) unsigned gu32;
#define LAS __attribute__((address_space(3)))
#define DI __device__ __forceinline__
#define RLX_AGENT __ATOMIC_RELAXED, __HIP_MEMORY_SCOPE_AGENT

constexpr int S_ = 2048, D_ = 1024, H_ = 32, F_ = 3584;
constexpr unsigned WS_XN = 0;
constexpr unsigned WS_AF = 4u << 20;
constexpr unsigned AF_BYTES = 16u * 144 * 8 * 1024;
constexpr unsigned WS_KVP = WS_AF + AF_BYTES;
constexpr unsigned KVP_STRIDE = 4352;
constexpr unsigned WS_CNT = 26u << 20;
constexpr unsigned WS_XCH = 27u << 20;
constexpr unsigned WS_USED = 32u << 20;
constexpr int CNT_Y = 0, CNT_X = 16, CNT_O = 32, CNT_KV = 48, CNT_LN = 64, CNT_IN = 72, CNT_EPOCH = 120;

DI unsigned cvtpk(float lo, float hi) {
  f32x2 v = {lo, hi};
  return __builtin_bit_cast(unsigned, __builtin_convertvector(v, bf16x2));
}
DI float bf_lo(unsigned u) { return __uint_as_float(u << 16); }
DI float bf_hi(unsigned u) { return __uint_as_float(u & 0xffff0000u); }
DI u32x4 make_srd(const void* p, unsigned bytes) {
  uint64_t a = (uint64_t)p;
  u32x4 r;
  r.x = (unsigned)a; r.y = (unsigned)(a >> 32) & 0xffffu; r.z = bytes; r.w = 0x00020000u;
  return r;
}
DI void dma16(unsigned ldsaddr, unsigned voff, u32x4 srd, unsigned soff) {
  asm volatile("s_mov_b32 m0, %0\n\ts_nop 0\n\tbuffer_load_dwordx4 %1, %2, %3 offen lds"
               ::"s"(ldsaddr), "v"(voff), "s"(srd), "s"(soff) : "memory");
}
DI void dma16_sc1(unsigned ldsaddr, unsigned voff, u32x4 srd, unsigned soff) {
  asm volatile("s_mov_b32 m0, %0\n\ts_nop 0\n\tbuffer_load_dwordx4 %1, %2, %3 offen sc1 lds"
               ::"s"(ldsaddr), "v"(voff), "s"(srd), "s"(soff) : "memory");
}
DI void dma16x2_sc1(unsigned ldsaddr, unsigned voff, u32x4 srd, unsigned soff) {
  asm volatile("s_mov_b32 m0, %0\n\ts_nop 0\n\tbuffer_load_dwordx4 %1, %2, %3 offen sc1 lds\n\t"
               "buffer_load_dwordx4 %1, %2, %3 offen offset:1024 sc1 lds"
               ::"s"(ldsaddr), "v"(voff), "s"(srd), "s"(soff) : "memory");
}
DI void touch_lds(unsigned ldsaddr, unsigned voff, u32x4 srd, unsigned soff) {
  asm volatile("s_mov_b32 m0, %0\n\ts_nop 0\n\tbuffer_load_dword %1, %2, %3 offen lds" ::"s"(ldsaddr), "v"(voff), "s"(srd), "s"(soff) : "memory");
}
DI bf16x8 tr2(LAS const char* p0, LAS const char* p1) {
  s16x4 a = __builtin_amdgcn_ds_read_tr16_b64_v4i16((LAS s16x4*)p0);
  s16x4 b = __builtin_amdgcn_ds_read_tr16_b64_v4i16((LAS s16x4*)p1);
  return __builtin_bit_cast(bf16x8, __builtin_shufflevector(a, b, 0, 1, 2, 3, 4, 5, 6, 7));
}
DI f32x4 mfma16(bf16x8 a, bf16x8 b, f32x4 c) { return __builtin_amdgcn_mfma_f32_16x16x32_bf16(a, b, c, 0, 0, 0); }
DI void mfma16a(f32x4& c, bf16x8 a, bf16x8 b) {
  asm volatile("v_mfma_f32_16x16x32_bf16 %0, %1, %2, %0" : "+v"(c) : "v"(a), "v"(b));
}
DI void mfma16m(f32x4& c, bf16x8 a, bf16x8 b) {
  asm volatile("v_mfma_f32_16x16x32_bf16 %0, %1, %2, %0" : "+v"(c) : "v"(a), "v"(b) : "memory");
}
DI bf16x8 pack8(f32x4 a, f32x4 b) {
  u32x4 w;
  w.x = cvtpk(a[0], a[1]); w.y = cvtpk(a[2], a[3]); w.z = cvtpk(b[0], b[1]); w.w = cvtpk(b[2], b[3]);
  return __builtin_bit_cast(bf16x8, w);
}
#define SCHED() __builtin_amdgcn_sched_barrier(0)

#define BAR() asm volatile("s_barrier" ::: "memory")
#define WAITV(n) asm volatile("s_waitcnt vmcnt(%0)" ::"n"(n) : "memory")
#define WAITL0() __builtin_amdgcn_s_waitcnt(0xC07F)
DI unsigned next_tag(unsigned e) { return e * 0x9E3779B1u + 0x7F4A7C15u; }
DI bool poll_tag(gu32* lane_word, unsigned tag) { return __all(__hip_atomic_load(lane_word, RLX_AGENT) == tag); }
DI void wait_tag(gu32* lane_word, unsigned tag) {
#pragma unroll 1
  for (unsigned spins = 0; spins < (1u << 20); ++spins) {
    if (poll_tag(lane_word, tag)) return;
    __builtin_amdgcn_s_sleep(1);
  }
}

DI float red_sum(float v) {
  auto a = __builtin_amdgcn_permlane16_swap(__float_as_uint(v), __float_as_uint(v), false, false);
  v = __uint_as_float(a[0]) + __uint_as_float(a[1]);
  auto b = __builtin_amdgcn_permlane32_swap(__float_as_uint(v), __float_as_uint(v), false, false);
  return __uint_as_float(b[0]) + __uint_as_float(b[1]);
}
template <int CTRL> DI float dpp_add(float v) {
  return v + __uint_as_float(__builtin_amdgcn_update_dpp(0u, __float_as_uint(v), CTRL, 0xf, 0xf, false));
}
constexpr int L1_A = 0;
constexpr int L1_B = 65536;
constexpr int L1_WPHI = 147456;
constexpr int L1_JUNK = 151552;
constexpr int L1_RED = 151808;
constexpr int L_TOTAL = 152320;
constexpr int D2 = 4;
constexpr int ST2 = 32768;
constexpr int L2_KV = D2 * ST2;
constexpr int NT2 = 36;
constexpr float L2E = 1.4426950408889634f;

DI float mx2(float a, float b) { return __builtin_elementwise_maximum(a, b); }
DI float max8(f32x4 a, f32x4 b) { return mx2(mx2(mx2(mx2(a[0], a[1]), a[2]), mx2(mx2(a[3], b[0]), b[1])), mx2(b[2], b[3])); }
template <bool MAX> DI float red4(float a0, float b0, float a1, float b1) {
  auto op = [](unsigned x, unsigned y) { return MAX ? mx2(__uint_as_float(x), __uint_as_float(y)) : __uint_as_float(x) + __uint_as_float(y); };
  const auto s0 = __builtin_amdgcn_permlane32_swap(__float_as_uint(a0), __float_as_uint(b0), false, false);
  const auto s1 = __builtin_amdgcn_permlane32_swap(__float_as_uint(a1), __float_as_uint(b1), false, false);
  const auto t = __builtin_amdgcn_permlane16_swap(__float_as_uint(op(s0[0], s0[1])), __float_as_uint(op(s1[0], s1[1])), false, false);
  return op(t[0], t[1]);
}
DI void bcast4(float r, float& a0, float& b0, float& a1, float& b1) {
  const auto u = __builtin_amdgcn_permlane16_swap(__float_as_uint(r), __float_as_uint(r), false, false);
  const auto w0 = __builtin_amdgcn_permlane32_swap(u[0], u[0], false, false);
  const auto w1 = __builtin_amdgcn_permlane32_swap(u[1], u[1], false, false);
  a0 = __uint_as_float(w0[0]); b0 = __uint_as_float(w0[1]); a1 = __uint_as_float(w1[0]); b1 = __uint_as_float(w1[1]);
}
DI f32x4 exp4(f32x4 p, float s, float o) {
  const f32x4 t = __builtin_elementwise_fma(p, (f32x4){s, s, s, s}, (f32x4){o, o, o, o});
  return (f32x4){__builtin_amdgcn_exp2f(t[0]), __builtin_amdgcn_exp2f(t[1]), __builtin_amdgcn_exp2f(t[2]), __builtin_amdgcn_exp2f(t[3])};
}
DI float sum8(f32x4 a, f32x4 b) {
  const f32x4 s = a + b;
  const f32x2 t = (f32x2){s[0], s[1]} + (f32x2){s[2], s[3]};
  return t[0] + t[1];
}
DI void dual_softmax2(f32x4 p00, f32x4 p01, f32x4 p10, f32x4 p11, bf16x8& pos0, bf16x8& neg0, bf16x8& pos1, bf16x8& neg1) {
  float a0, b0, a1, b1;
  bcast4(-L2E * red4<true>(max8(p00, p01), max8(-p00, -p01), max8(p10, p11), max8(-p10, -p11)), a0, b0, a1, b1);
  const f32x4 e00 = exp4(p00, L2E, a0), e01 = exp4(p01, L2E, a0), f00 = exp4(p00, -L2E, b0), f01 = exp4(p01, -L2E, b0);
  const f32x4 e10 = exp4(p10, L2E, a1), e11 = exp4(p11, L2E, a1), f10 = exp4(p10, -L2E, b1), f11 = exp4(p11, -L2E, b1);
  bcast4(__builtin_amdgcn_rcpf(red4<false>(sum8(e00, e01), sum8(f00, f01), sum8(e10, e11), sum8(f10, f11))), a0, b0, a1, b1);
  pos0 = pack8(e00 * a0, e01 * a0); neg0 = pack8(f00 * b0, f01 * b0);
  pos1 = pack8(e10 * a1, e11 * a1); neg1 = pack8(f10 * b1, f11 * b1);
}
DI f32x4 glu4(f32x4 g, f32x4 u) {
  const f32x4 t = g * -L2E;
  const f32x4 d = (f32x4){__builtin_amdgcn_exp2f(t[0]), __builtin_amdgcn_exp2f(t[1]), __builtin_amdgcn_exp2f(t[2]), __builtin_amdgcn_exp2f(t[3])} + 1.0f;
  return u * (f32x4){__builtin_amdgcn_rcpf(d[0]), __builtin_amdgcn_rcpf(d[1]), __builtin_amdgcn_rcpf(d[2]), __builtin_amdgcn_rcpf(d[3])};
}

struct Args {
  const u16 *x, *wqkv, *wphi, *wo, *wgate, *wup, *wdown;
  char* ws;
  u16* out;
};

struct P2 {
  int kh, rt, ct;
  unsigned ldsb, vA, vB;
  int pw;
  u32x4 srdA, srdD, srdO;
  DI void init(const Args& a, unsigned ldsb_, int pw_, int lane) {
    const int bid = blockIdx.x, xcd = bid & 7, idx = bid >> 3, xq = xcd >> 1;
    kh = xcd & 1; rt = 8 * (xq >> 1) + (idx & 7); ct = 4 * (xq & 1) + (idx >> 3);
    ldsb = ldsb_; pw = pw_;
    srdA = make_srd(a.ws + WS_AF, AF_BYTES);
    srdD = make_srd(a.wdown, F_ * D_ * 2);
    srdO = make_srd(a.wo, D_ * D_ * 2);
    vA = (unsigned)rt * (144u * 8192u) + pw * 4096 + lane * 16;
    const int kl = 16 * pw + (lane >> 3);
    const int cp = ((lane & 7) >> 1) ^ ((kl >> 1) & 3);
    vB = (unsigned)kl * 2048u + (unsigned)(128 * ct + 16 * cp + 8 * (lane & 1)) * 2u;
  }
  DI int gt(int t) const { return t < 16 ? 16 * kh + t : t < 28 ? 16 + 12 * kh + t : 28 + 8 * kh + t; }
  DI void issueA(int t, int slot) const {
    const unsigned base = ldsb + slot * ST2 + pw * 4096, sa = gt(t) * 16384;
    dma16x2_sc1(base, vA, srdA, sa + kh * 2048);
    dma16x2_sc1(base + 2048, vA, srdA, sa + 2048 - kh * 2048);
  }
  DI void issueB(int t, int slot) const {
    const unsigned base = ldsb + slot * ST2 + 16384 + pw * 2048;
    const int g = gt(t);
    const u32x4 sb = g < 56 ? srdD : srdO;
    const unsigned so = (g < 56 ? g : g - 56) * 131072;
    dma16(base, vB, sb, so);
    dma16(base + 8192, vB, sb, so + 128);
    dma16(base + 1024, vB, sb, so + 16384);
    dma16(base + 8192 + 1024, vB, sb, so + 16384 + 128);
  }
};

DI unsigned phase0(LAS char* lds, const Args& a, const int p, const int cc) {
  char* ws = a.ws;
  const int tid = threadIdx.x, lane = tid & 63;
  const int wave = __builtin_amdgcn_readfirstlane(tid >> 6);
  const int iw = wave & 3, type = cc & 1, j = cc >> 1;
  const unsigned ldsb = (unsigned)(size_t)lds;
  gu32* ln = (gu32*)(ws + WS_CNT) + 64 * (CNT_LN + p);
  unsigned tag;
  unsigned long long pc;
  unsigned code_bytes;
  asm volatile("s_getpc_b64 %0\n.Lk_main_pc:\n\ts_mov_b32 %1, .Lk_main_late-.Lk_main_pc" : "=s"(pc), "=s"(code_bytes));
  if (__builtin_expect(wave < 4, 1)) {
    const int r = lane & 7, c8 = lane >> 3;
    const u16* xp = a.x + (size_t)(256 * p + 8 * cc + r) * D_ + wave * 256 + c8 * 8;
    u32x4 xv[4];
#pragma unroll
    for (int i = 0; i < 4; ++i) xv[i] = *(const u32x4*)(xp + i * 64);
    asm volatile("" ::: "memory");
    const unsigned ep = __hip_atomic_load((gu32*)(ws + WS_CNT) + 64 * CNT_EPOCH, RLX_AGENT);
    float f[32];
#pragma unroll
    for (int i = 0; i < 4; ++i)
#pragma unroll
      for (int e = 0; e < 4; ++e) { f[i * 8 + 2 * e] = bf_lo(xv[i][e]); f[i * 8 + 2 * e + 1] = bf_hi(xv[i][e]); }
    float s0 = 0.f, s1 = 0.f, s2 = 0.f, s3 = 0.f;
#pragma unroll
    for (int i = 0; i < 8; ++i) { s0 += f[i]; s1 += f[8 + i]; s2 += f[16 + i]; s3 += f[24 + i]; }
    const float mw = red_sum(dpp_add<0x128>((s0 + s1) + (s2 + s3))) * (1.0f / 256);
    s0 = s1 = s2 = s3 = 0.f;
#pragma unroll
    for (int i = 0; i < 8; ++i) {
      const float d0 = f[i] - mw, d1 = f[8 + i] - mw, d2 = f[16 + i] - mw, d3 = f[24 + i] - mw;
      s0 += d0 * d0; s1 += d1 * d1; s2 += d2 * d2; s3 += d3 * d3;
    }
    const float m2 = red_sum(dpp_add<0x128>((s0 + s1) + (s2 + s3)));
    LAS f32x2* red = (LAS f32x2*)(lds + L1_RED);
    if (lane < 8) red[wave * 8 + lane] = (f32x2){mw, m2};
    WAITL0();
    BAR();
    const f32x2 p0 = red[r], p1 = red[8 + r], p2 = red[16 + r], p3 = red[24 + r];
    const float mean = ((p0.x + p1.x) + (p2.x + p3.x)) * 0.25f;
    const float d0 = p0.x - mean, d1 = p1.x - mean, d2 = p2.x - mean, d3 = p3.x - mean;
    const float var = (((p0.y + p1.y) + (p2.y + p3.y)) + 256.0f * ((d0 * d0 + d1 * d1) + (d2 * d2 + d3 * d3))) * (1.0f / D_);
    const float rstd = 1.0f / sqrtf(var + 1e-5f);
    const auto rxn = __builtin_amdgcn_make_buffer_rsrc(ws + WS_XN, 0, 4u << 20, 0x00020000);
    const unsigned dst = (((unsigned)(16 * p + (cc >> 1)) * 32 + 8 * wave + (c8 >> 2)) * 64 + (c8 & 3) * 16 + (cc & 1) * 8 + r) * 16;
#pragma unroll
    for (int i = 0; i < 4; ++i) {
      u32x4 o;
#pragma unroll
      for (int e = 0; e < 4; ++e) o[e] = cvtpk((f[i * 8 + 2 * e] - mean) * rstd, (f[i * 8 + 2 * e + 1] - mean) * rstd);
      __builtin_amdgcn_raw_buffer_store_b128(o, rxn, dst + i * 2048, 0, 16);
    }
    WAITV(0);
    tag = next_tag(__builtin_amdgcn_readfirstlane(ep));
    if (tid == 0) *(LAS unsigned*)(lds + L1_RED + 256) = tag;
    WAITL0();
    BAR();
    if (tid == 0) __hip_atomic_store(ln + cc, tag, RLX_AGENT);
  } else {
    const u32x4 srdQ = make_srd(a.wqkv, D_ * 3 * D_ * 2);
    const u32x4 srdG = make_srd(a.wgate, D_ * F_ * 2);
    const u32x4 srdU = make_srd(a.wup, D_ * F_ * 2);
    const int kl = 8 * iw + (lane >> 3);
    const int cpair = ((lane & 7) >> 1) ^ (((lane >> 4) & 1) | ((iw & 1) << 1));
    const int colo = 16 * cpair + 8 * (lane & 1);
    const unsigned vQ = (unsigned)kl * 6144u + (unsigned)((type == 0 ? 1024 : 0) + 64 * j + colo) * 2u;
    const int fbase = type == 0 ? 2048 + 96 * j : 128 * j;
    const unsigned vG = (unsigned)kl * 7168u + (unsigned)(fbase + colo) * 2u;
    const int kl4 = 16 * (iw & 1) + (lane >> 2);
    const int c4 = ((lane & 3) >> 1) ^ ((lane >> 5) & 1);
    const unsigned v4 = (unsigned)kl4 * 7168u + (unsigned)(fbase + 64 + 16 * c4 + 8 * (lane & 1)) * 2u;
    const unsigned v1 = type ? vG : vQ + 2048, v3 = type ? vG + 128 : vG, v5 = type ? vG + 128 : v4;
    const u32x4 s1 = type ? srdG : srdQ, s2 = type ? srdU : srdG, s3 = type ? srdG : srdU, s5 = (type == 0 && iw < 2) ? srdG : srdU;
    {
      touch_lds(ldsb + L1_JUNK, (unsigned)((((blockIdx.x >> 3) & 3) * 4 + iw) * 4096 + lane * 64), make_srd((const void*)pc, code_bytes), 0);
    }
#pragma unroll 1
    for (int u = 0; u < 3; ++u) {
      const unsigned base = ldsb + L1_B + u * 20480 + iw * 1024;
      const unsigned sq = u * (32 * 6144), sg = u * (32 * 7168);
      dma16(base, vQ, srdQ, sq);
      dma16(base + 4096, v1, s1, type ? sg : sq);
      dma16(base + 8192, vG, s2, sg);
      dma16(base + 12288, v3, s3, sg);
      dma16(base + 16384, v5, s5, sg);
    }
    dma16(ldsb + L1_WPHI + iw * 1024, lane * 16, make_srd(a.wphi, H_ * 32 * 32 * 2), (unsigned)(2 * j) * 2048u + iw * 1024);
    BAR();
    BAR();
    tag = __builtin_amdgcn_readfirstlane(*(LAS const unsigned*)(lds + L1_RED + 256));
    const auto rx = __builtin_amdgcn_make_buffer_rsrc(ws + WS_XCH, 0, 256u * 16384u, 0x00020000);
    const u32x4 ones = {0xffffffffu, 0xffffffffu, 0xffffffffu, 0xffffffffu};
#pragma unroll
    for (int k = 0; k < 4; ++k) __builtin_amdgcn_raw_buffer_store_b128(ones, rx, (blockIdx.x ^ 1) * 16384u + iw * 4096 + lane * 16 + k * 1024, 0, 16);
    const unsigned wq = blockIdx.x * 4 + iw;
#pragma unroll
    for (int k = 0; k < 2; ++k) touch_lds(ldsb + L1_JUNK, lane < 48 ? wq * 3072u + lane * 64 : 0x40000000u, srdQ, k * (1024u * 3072u));
  }
  return tag;
}

template <int TYPE  , int G>
DI void phase1(LAS char* lds, const Args& a, const int p, const int j, const unsigned tag, u32x2 (&xr)[4][2], bf16x8 (&ph)[4][2]) {
  char* ws = a.ws;
  const int wave = __builtin_amdgcn_readfirstlane(threadIdx.x >> 6);
  const int iw = wave & 3, wri = iw >> 1, wc = iw & 1;
  const unsigned ldsb = (unsigned)(size_t)lds;
  f32x4 acc[10][4];
  {
    const int tid = threadIdx.x, lane = tid & 63;
    const int fq = lane >> 4;
    const u32x4 srdX = make_srd(ws + WS_XN, 4u << 20);
    const u32x4 srdQ = make_srd(a.wqkv, D_ * 3 * D_ * 2);
    const u32x4 srdG = make_srd(a.wgate, D_ * F_ * 2);
    const u32x4 srdU = make_srd(a.wup, D_ * F_ * 2);
    const unsigned vA = lane * 16 + (unsigned)(16 * p + 2 * iw) * 32768u;
    const int kl = 8 * iw + (lane >> 3);
    const int cpair = ((lane & 7) >> 1) ^ (((lane >> 4) & 1) | ((iw & 1) << 1));
    const int colo = 16 * cpair + 8 * (lane & 1);
    const unsigned vQ = (unsigned)kl * 6144u + (unsigned)((TYPE == 0 ? 1024 : 0) + 64 * j + colo) * 2u;
    const int fbase = TYPE == 0 ? 2048 + 96 * j : 128 * j;
    const unsigned vG = (unsigned)kl * 7168u + (unsigned)(fbase + colo) * 2u;
    const int kl4 = 16 * (iw & 1) + (lane >> 2);
    const int c4 = ((lane & 3) >> 1) ^ ((lane >> 5) & 1);
    const unsigned v4 = (unsigned)kl4 * 7168u + (unsigned)(fbase + 64 + 16 * c4 + 8 * (lane & 1)) * 2u;
    const u32x4 srd4 = (iw < 2) ? srdG : srdU;
    const unsigned vW = lane < 6 ? (unsigned)(blockIdx.x * 4 + iw) * 384u + lane * 64 : 0x40000000u;
    const u32x4 srdD = make_srd(a.wdown, F_ * D_ * 2), srdO = make_srd(a.wo, D_ * D_ * 2);
    auto warm = [&](int u) { touch_lds(ldsb + L1_JUNK, vW, u < 19 ? srdD : srdO, (u < 19 ? u : u - 19) * 393216u); };
    auto pieceA = [&](int t, int g, int k) {
      const unsigned base = ldsb + L1_A + (t & 1) * 32768 + g * 16384 + iw * 4096;
      dma16_sc1(base + k * 1024, vA, srdX, g * 262144 + t * 2048 + (k & 1) * 1024 + (k >> 1) * 32768);
    };
    auto pieceB = [&](int t, int h, int k) {
      const unsigned base = ldsb + L1_B + ((t & 1) * 2 + h) * 20480 + iw * 1024 + k * 4096;
      const unsigned sq = (2 * t + h) * (32 * 6144), sg = (2 * t + h) * (32 * 7168);
      if (TYPE == 0) {
        if (k == 0) dma16(base, vQ, srdQ, sq);
        if (k == 1) dma16(base, vQ, srdQ, sq + 2048);
        if (k == 2) dma16(base, vG, srdG, sg);
        if (k == 3) dma16(base, vG, srdU, sg);
        if (k == 4) dma16(base, v4, srd4, sg);
      } else {
        if (k == 0) dma16(base, vQ, srdQ, sq);
        if (k == 1) dma16(base, vG, srdG, sg);
        if (k == 2) dma16(base, vG, srdU, sg);
        if (k == 3) dma16(base, vG, srdG, sg + 128);
        if (k == 4) dma16(base, vG, srdU, sg + 128);
      }
    };
    auto issueA = [&](int t) {
#pragma unroll
      for (int k = 0; k < 4; ++k) pieceA(t, 1 - G, k);
    };
    if (G == 0 && wave == 0) wait_tag((gu32*)(ws + WS_CNT) + 64 * (CNT_LN + p) + (lane & 31), tag);
    BAR();
    issueA(0);
    if (G == 1) issueA(1);
    const int q4 = (lane & 15) >> 2, p4 = lane & 3;
    const int swz = ((q4 >> 1) & 1) | ((fq & 1) << 1);
    LAS const char* pA = lds + L1_A + G * 16384 + wri * 8192 + lane * 16;
    LAS const char* pB0l = lds + L1_B + 128 * (8 * fq + q4) + 32 * ((2 * wc) ^ swz) + 8 * p4;
    LAS const char* pB1l = lds + L1_B + 128 * (8 * fq + q4) + 32 * ((2 * wc + 1) ^ swz) + 8 * p4;
    LAS const char* pB4l = lds + L1_B + 16384 + 64 * (8 * fq + q4) + 32 * (wc ^ (fq & 1)) + 8 * p4;
    LAS const char* pB0h = pB0l + 40960;
    LAS const char* pB1h = pB1l + 40960;
    LAS const char* pB4h = pB4l + 40960;
    asm volatile("" : "+v"(pB0l), "+v"(pB1l), "+v"(pB0h), "+v"(pB1h));
    if (TYPE == 0) asm volatile("" : "+v"(pB4l), "+v"(pB4h));
#pragma unroll
    for (int n = 0; n < 10; ++n)
#pragma unroll
      for (int i = 0; i < 4; ++i) { acc[n][i] = (f32x4){0.f, 0.f, 0.f, 0.f}; asm volatile("" : "+v"(acc[n][i])); }
    bf16x8 Af[4], Bf[10];
    if (G == 0) WAITV(0); else WAITV(4);
    BAR();
    if (G == 1) BAR();

    auto Lphase = [&](auto bufc, auto ksc, int t, auto modec) {
      constexpr int buf = decltype(bufc)::value, ks = decltype(ksc)::value, mode = decltype(modec)::value;
      constexpr int bo = ks * 20480;
      LAS const char* pB0 = buf ? pB0h : pB0l;
      LAS const char* pB1 = buf ? pB1h : pB1l;
      LAS const char* pB4 = buf ? pB4h : pB4l;
      auto piece = [&](int k) {
        if (mode == 0 || mode == 2) {
          if (G == 1) { if (k < 4) pieceA(t + 1 + ks, 1 - ks, k); } else pieceB(t + 1 + ks, 1 - ks, k);
        }
      };
      __builtin_amdgcn_s_setprio(2);
      piece(0);
#pragma unroll
      for (int i = 0; i < 4; ++i) Af[i] = *(LAS const bf16x8*)(pA + buf * 32768 + i * 2048 + ks * 1024);
#pragma unroll
      for (int b = 0; b < 4; ++b) {
        piece(b + 1);
        Bf[2 * b] = tr2(pB0 + bo + b * 4096, pB0 + bo + b * 4096 + 512);
        Bf[2 * b + 1] = tr2(pB1 + bo + b * 4096, pB1 + bo + b * 4096 + 512);
      }
      if (TYPE == 0) {
        Bf[8] = tr2(pB4 + bo, pB4 + bo + 256);
        Bf[9] = tr2(pB4 + bo + 2048, pB4 + bo + 2048 + 256);
      } else {
        Bf[8] = tr2(pB0 + bo + 16384, pB0 + bo + 16384 + 512);
        Bf[9] = tr2(pB1 + bo + 16384, pB1 + bo + 16384 + 512);
      }
      if (mode == 0 && G == 0) warm(2 * t + ks);
      WAITL0();
      BAR();
      SCHED();
    };
    auto Mphase = [&](auto wc_, bool bar = true) {
      constexpr int wn_ = decltype(wc_)::value;
      __builtin_amdgcn_s_setprio(0);
#pragma unroll
      for (int n = 0; n < 10; ++n)
#pragma unroll
        for (int i = 0; i < 4; ++i) mfma16a(acc[n][i], Bf[n], Af[i]);
      WAITV(wn_);
      if (bar) BAR();
      SCHED();
    };
    using WS_ = std::integral_constant<int, G == 0 ? 13 : 4>;
    using I0 = std::integral_constant<int, 0>;
    using I1 = std::integral_constant<int, 1>;
    for (int t = 0; t < 14; t += 2) {
      Lphase(I0{}, I0{}, t, I0{}); Mphase(WS_{});
      Lphase(I0{}, I1{}, t, I0{}); Mphase(WS_{});
      Lphase(I1{}, I0{}, t + 1, I0{}); Mphase(WS_{});
      Lphase(I1{}, I1{}, t + 1, I0{}); Mphase(WS_{});
    }
    Lphase(I0{}, I0{}, 14, std::integral_constant<int, 2>{}); Mphase(std::integral_constant<int, G == 0 ? 12 : 4>{});
    Lphase(I0{}, I1{}, 14, I1{}); Mphase(std::integral_constant<int, G == 0 ? 6 : 0>{});
    Lphase(I1{}, I0{}, 15, I1{}); Mphase(I0{});
    Lphase(I1{}, I1{}, 15, I1{}); Mphase(I0{}, G == 0);
    asm volatile("s_nop 15" ::: "memory");
    SCHED();
  }
  int tz = threadIdx.x;
  asm volatile("" : "+v"(tz));
  const int lane = tz & 63;
  const int fr = lane & 15, fq = lane >> 4;
  const int q4 = (lane & 15) >> 2, p4 = lane & 3;
  gu32* cnt = (gu32*)(ws + WS_CNT);
  P2 g2;
  g2.init(a, ldsb, iw, lane);
  gu32* cntY = cnt + 64 * (CNT_Y + (g2.rt >> 1) * 2 + g2.kh) + (lane & 7);
  const int rbw = 16 * p + 8 * G + 4 * wri;
  const auto rws = __builtin_amdgcn_make_buffer_rsrc(ws, 0, WS_USED, 0x00020000);
  const unsigned afb = WS_AF + ((unsigned)(rbw >> 3) * 144 * 8 + (rbw & 7)) * 1024 + lane * 16;
#define ST16(off, v) __builtin_amdgcn_raw_buffer_store_b128(__builtin_bit_cast(u32x4, v), rws, off, 0, 16)
  if (G == 1) {
    const u16* xp = a.x + (size_t)(128 * g2.rt + 64 * wri + 32 * g2.kh + fr) * D_ + 128 * g2.ct + 64 * wc + 4 * fq;
#pragma unroll
    for (int n = 0; n < 4; ++n)
#pragma unroll
      for (int e = 0; e < 2; ++e) xr[n][e] = *(const u32x2*)(xp + e * 16 * D_ + 16 * n);
    asm volatile("" ::: "memory");
  }
  bf16x8 Wp[2];
  {
    LAS const char* pw = lds + L1_WPHI + wc * 2048 + 64 * (4 * fq + q4) + 8 * p4;
    Wp[0] = tr2(pw, pw + 1024);
    Wp[1] = tr2(pw + 32, pw + 1024 + 32);
  }
  const f32x4 z4 = {0.f, 0.f, 0.f, 0.f};
  auto feature_map = [&](int i) {
    const bf16x8 f0 = pack8(acc[0][i], acc[1][i]), f1 = pack8(acc[0][i + 1], acc[1][i + 1]);
    dual_softmax2(mfma16(Wp[0], f0, z4), mfma16(Wp[1], f0, z4), mfma16(Wp[0], f1, z4), mfma16(Wp[1], f1, z4), ph[i][0], ph[i][1], ph[i + 1][0], ph[i + 1][1]);
  };
  if (TYPE == 1) {
    if (G == 0) {
#pragma unroll
      for (int t = 0; t < D2; ++t) g2.issueB(t, t);
    }
    const int ksb = 4 * j;
#pragma unroll
    for (int i = 0; i < 4; ++i) {
      const bf16x8 h0 = pack8(glu4(acc[2][i], acc[4][i]), glu4(acc[3][i], acc[5][i]));
      ST16(afb + (ksb + wc) * 8192 + i * 1024, h0);
      const bf16x8 h1 = pack8(glu4(acc[6][i], acc[8][i]), glu4(acc[7][i], acc[9][i]));
      ST16(afb + (ksb + 2 + wc) * 8192 + i * 1024, h1);
    }
    SCHED();
    feature_map(0);
    asm volatile("" ::"v"(ph[0][0]), "v"(ph[0][1]), "v"(ph[1][0]), "v"(ph[1][1]));
    SCHED();
    WAITV(0);
    BAR();
    if (tz == 0) __hip_atomic_store(cnt + 64 * (CNT_Y + p * 2 + (j >> 3)) + (j & 7), tag, RLX_AGENT);
    feature_map(2);
    asm volatile("" ::"v"(ph[2][0]), "v"(ph[2][1]), "v"(ph[3][0]), "v"(ph[3][1]));
    SCHED();
    if (G == 0) {
      wait_tag(cntY, tag);
#pragma unroll
      for (int t = 0; t < D2; ++t) g2.issueA(t, t);
    }
  } else {
    constexpr int PH = 0, VV = 65536;
    const int fm = (((fr >> 1) & 3) << 2) | ((fr & 1) << 1) | ((fr >> 3) & 1);
    const int fv = (((fr >> 2) & 1) << 2) | (((fr >> 1) & 1) << 1) | ((fr >> 3) & 1);
    feature_map(0);
    feature_map(2);
    {
      const int m0 = 128 * G + 64 * wri + fr;
      LAS char* w0 = lds + PH + wc * 32768 + m0 * 128 + 8 * ((0 + fq) ^ fm);
      LAS char* w1 = lds + PH + wc * 32768 + m0 * 128 + 8 * ((4 + fq) ^ fm);
      LAS char* w2 = lds + PH + wc * 32768 + m0 * 128 + 8 * ((8 + fq) ^ fm);
      LAS char* w3 = lds + PH + wc * 32768 + m0 * 128 + 8 * ((12 + fq) ^ fm);
      LAS char* u0 = lds + VV + wc * 16384 + m0 * 64 + 8 * ((0 + fq) ^ fv);
      LAS char* u1 = lds + VV + wc * 16384 + m0 * 64 + 8 * ((4 + fq) ^ fv);
      asm volatile("" : "+v"(w0), "+v"(w1), "+v"(w2), "+v"(w3), "+v"(u0), "+v"(u1));
#pragma unroll
      for (int i = 0; i < 4; ++i) {
        const u32x4 pw = __builtin_bit_cast(u32x4, ph[i][0]), nw = __builtin_bit_cast(u32x4, ph[i][1]);
        *(LAS u32x2*)(w0 + i * 2048) = (u32x2){pw.x, pw.y};
        *(LAS u32x2*)(w1 + i * 2048) = (u32x2){pw.z, pw.w};
        *(LAS u32x2*)(w2 + i * 2048) = (u32x2){nw.x, nw.y};
        *(LAS u32x2*)(w3 + i * 2048) = (u32x2){nw.z, nw.w};
        *(LAS u32x2*)(u0 + i * 1024) = (u32x2){cvtpk(acc[2][i][0], acc[2][i][1]), cvtpk(acc[2][i][2], acc[2][i][3])};
        *(LAS u32x2*)(u1 + i * 1024) = (u32x2){cvtpk(acc[3][i][0], acc[3][i][1]), cvtpk(acc[3][i][2], acc[3][i][3])};
      }
    }
    LAS char* h1p = lds + L2_KV + (8 * G + 4 * wri) * 1024 + lane * 16 + wc * 8;
    auto gluX = [&](int i) {
      ph[i][0] = pack8(glu4(acc[4][i], acc[6][i]), glu4(acc[5][i], acc[7][i]));
      const f32x4 h1 = glu4(acc[8][i], acc[9][i]);
      *(LAS u32x2*)(h1p + i * 1024) = (u32x2){cvtpk(h1[0], h1[1]), cvtpk(h1[2], h1[3])};
      asm volatile("" : "+v"(ph[i][0]));
    };
    SCHED();
    WAITL0();
    BAR();
    if (G == 0) {
      gluX(0); gluX(1); gluX(2); gluX(3);
      SCHED();
      wait_tag(cntY, tag);
      BAR();
#pragma unroll
      for (int t = 0; t < D2; ++t) { g2.issueB(t, t); g2.issueA(t, t); }
    } else {
      const int hh = iw >> 1, hf = iw & 1;
      const int mr = 4 * fq + q4;
      const int fm2 = (((mr >> 1) & 3) << 2) | ((mr & 1) << 1) | ((mr >> 3) & 1);
      const int fv2 = (((mr >> 2) & 1) << 2) | (((mr >> 1) & 1) << 1) | ((mr >> 3) & 1);
      LAS const char* rp0 = lds + PH + hh * 32768 + mr * 128 + 8 * ((8 * hf + p4) ^ fm2);
      LAS const char* rp1 = lds + PH + hh * 32768 + mr * 128 + 8 * ((8 * hf + 4 + p4) ^ fm2);
      LAS const char* rv0 = lds + VV + hh * 16384 + mr * 64 + 8 * ((0 + p4) ^ fv2);
      LAS const char* rv1 = lds + VV + hh * 16384 + mr * 64 + 8 * ((4 + p4) ^ fv2);
      asm volatile("" : "+v"(rp0), "+v"(rp1), "+v"(rv0), "+v"(rv1));
      const u32x4 onesw = {0x3f803f80u, 0x3f803f80u, 0x3f803f80u, 0x3f803f80u};
      const bf16x8 ones = __builtin_bit_cast(bf16x8, onesw);
      __builtin_amdgcn_s_setprio(1);
      f32x4 d00 = z4, d01 = z4, d0z = z4, d10 = z4, d11 = z4, d1z = z4;
#pragma unroll
      for (int s = 0; s < 8; ++s) {
        const bf16x8 a0 = tr2(rp0 + s * 4096, rp0 + s * 4096 + 2048);
        const bf16x8 a1 = tr2(rp1 + s * 4096, rp1 + s * 4096 + 2048);
        const bf16x8 b0 = tr2(rv0 + s * 2048, rv0 + s * 2048 + 1024);
        const bf16x8 b1 = tr2(rv1 + s * 2048, rv1 + s * 2048 + 1024);
        d00 = mfma16(a0, b0, d00); d01 = mfma16(a0, b1, d01); d0z = mfma16(a0, ones, d0z);
        d10 = mfma16(a1, b0, d10); d11 = mfma16(a1, b1, d11); d1z = mfma16(a1, ones, d1z);
      }
      const unsigned kv = WS_KVP + (unsigned)((2 * j + hh) * 8 + p) * KVP_STRIDE;
      ST16(kv + hf * 1024 + lane * 16, pack8(d00, d10));
      ST16(kv + (2 + hf) * 1024 + lane * 16, pack8(d01, d11));
      ST16(fr < 2 ? kv + 4096 + (2 * hf + fr) * 64 + fq * 16 : 0x40000000u, fr ? d1z : d0z);
      __builtin_amdgcn_s_setprio(0);
      SCHED();
      gluX(0); gluX(1);
      SCHED();
      BAR();
      gluX(2); gluX(3);
      SCHED();
      WAITV(0);
      if (lane == 0) __hip_atomic_store(cnt + 64 * (CNT_KV + j) + p * 4 + iw, tag, RLX_AGENT);
    }
    WAITL0();
  }
}

DI bf16x8 o_block(const bf16x8 (&kf)[3][2], bf16x8 pos, bf16x8 neg) {
  const f32x4 z4 = {0.f, 0.f, 0.f, 0.f};
  const f32x4 o0 = mfma16(kf[0][1], neg, mfma16(kf[0][0], pos, z4));
  const f32x4 o1 = mfma16(kf[1][1], neg, mfma16(kf[1][0], pos, z4));
  const f32x4 dn = mfma16(kf[2][1], neg, mfma16(kf[2][0], pos, z4));
  const float r = __builtin_amdgcn_rcpf(dn[0] + 1e-6f);
  return pack8(o0 * r, o1 * r);
}

DI void producer2(LAS char* lds, const Args& a, const int type, const int p, const int j, bf16x8 (&ph)[4][2]) {
  char* ws = a.ws;
  int tz = threadIdx.x;
  asm volatile("" : "+v"(tz));
  const int lane = tz & 63, iw = __builtin_amdgcn_readfirstlane(tz >> 6) & 3;
  P2 g2;
  g2.init(a, (unsigned)(size_t)lds, iw, lane);
  auto tile = [&](int t) { g2.issueA(t, t & 3); g2.issueB(t, t & 3); };
  int t = 4;
  const auto rws = __builtin_amdgcn_make_buffer_rsrc(ws, 0, WS_USED, 0x00020000);
  if (type) {
    WAITV(12); BAR();
    WAITV(8); BAR(); tile(4);
    WAITV(12); BAR(); tile(5);
    WAITV(16); BAR(); tile(6);
    WAITV(16); BAR(); tile(7);
  } else {
    const int rbw = 16 * p + 4 * (iw >> 1), rbh = 16 * p + 4 * iw;
    const unsigned o0 = WS_AF + ((unsigned)(rbw >> 3) * 144 * 8 + (rbw & 7)) * 1024 + lane * 16 + (64 + 3 * j + (iw & 1)) * 8192;
    const unsigned o1 = WS_AF + ((unsigned)(rbh >> 3) * 144 * 8 + (rbh & 7)) * 1024 + lane * 16 + (64 + 3 * j + 2) * 8192;
    WAITV(24); BAR();
#pragma unroll
    for (int i = 0; i < 4; ++i) ph[i][1] = *(LAS const bf16x8*)(lds + L2_KV + (4 * iw + i) * 1024 + lane * 16);
    WAITV(16); BAR(); tile(4);
#pragma unroll
    for (int i = 0; i < 4; ++i) ST16(o0 + i * 1024, ph[i][0]);
    WAITV(20); BAR(); tile(5);
#pragma unroll
    for (int i = 0; i < 4; ++i) ST16(o1 + i * 1024, ph[i][1]);
    WAITV(24); BAR(); tile(6);
    WAITV(24); BAR(); tile(7);
    WAITV(20); BAR(); tile(8);
    t = 5;
  }
  if (type) {
    const int fr = lane & 15, fq = lane >> 4, wri = iw >> 1, wc = iw & 1;
    const u32x4 srdS = make_srd(ws, WS_USED);
    const unsigned vk = WS_KVP + (unsigned)(2 * j) * 8 * KVP_STRIDE + iw * 1024 + lane * 16;
    const unsigned vz = WS_KVP + (unsigned)(2 * j) * 8 * KVP_STRIDE + (fr & 7) * KVP_STRIDE + 4096 + iw * 64 + fq * 16;
    u32x4 ka[9], kb[9];
    auto kvload = [&](u32x4 (&k)[9], int hh) {
#pragma unroll
      for (int pp = 0; pp < 8; ++pp)
        asm volatile("buffer_load_dwordx4 %0, %1, %2, %3 offen sc1" : "=v"(k[pp]) : "v"(vk), "s"(srdS), "s"((hh * 8 + pp) * KVP_STRIDE) : "memory");
      asm volatile("buffer_load_dwordx4 %0, %1, %2, %3 offen sc1" : "=v"(k[8]) : "v"(vz), "s"(srdS), "s"(hh * 8 * KVP_STRIDE) : "memory");
    };
    auto kvsum = [&](u32x4 (&k)[9], int hh) {
      asm volatile("" : "+v"(k[0]), "+v"(k[1]), "+v"(k[2]), "+v"(k[3]), "+v"(k[4]), "+v"(k[5]), "+v"(k[6]), "+v"(k[7]), "+v"(k[8]));
      f32x4 s0 = {bf_lo(k[0].x), bf_hi(k[0].x), bf_lo(k[0].y), bf_hi(k[0].y)}, s1 = {bf_lo(k[0].z), bf_hi(k[0].z), bf_lo(k[0].w), bf_hi(k[0].w)};
#pragma unroll
      for (int pp = 1; pp < 8; ++pp) {
        s0 += (f32x4){bf_lo(k[pp].x), bf_hi(k[pp].x), bf_lo(k[pp].y), bf_hi(k[pp].y)};
        s1 += (f32x4){bf_lo(k[pp].z), bf_hi(k[pp].z), bf_lo(k[pp].w), bf_hi(k[pp].w)};
      }
      f32x4 z = __builtin_bit_cast(f32x4, k[8]);
#pragma unroll
      for (int r = 0; r < 4; ++r) z[r] = dpp_add<0x141>(dpp_add<0x4E>(dpp_add<0xB1>(z[r])));
      *(LAS bf16x8*)(lds + L2_KV + hh * 6144 + iw * 1024 + lane * 16) = pack8(s0, s1);
      *(LAS u32x2*)(lds + L2_KV + hh * 6144 + (4 + (iw >> 1)) * 1024 + lane * 16 + (iw & 1) * 8) = (u32x2){cvtpk(z[0], z[1]), cvtpk(z[2], z[3])};
    };
    const int rbw = 16 * p + 4 * wri;
    const unsigned ofb = WS_AF + ((unsigned)(rbw >> 3) * 144 * 8 + (rbw & 7)) * 1024 + lane * 16 + (112 + 2 * j + wc) * 8192;
    for (; t < 8; ++t) { WAITV(16); BAR(); tile(t + D2); }
    WAITV(16); BAR(); tile(12); kvload(ka, 0);
    WAITV(25); BAR(); tile(13); kvload(kb, 1);
    WAITV(34); BAR(); tile(14);
    WAITV(34); BAR(); tile(15);
    WAITV(25); BAR(); tile(16); kvsum(ka, 0);
    SCHED();
    WAITV(16); BAR(); tile(17); kvsum(kb, 1);
    SCHED();
    WAITL0();
    WAITV(16); BAR(); tile(18);
    bf16x8 kf[3][2];
#pragma unroll
    for (int jd = 0; jd < 3; ++jd)
#pragma unroll
      for (int hf = 0; hf < 2; ++hf) kf[jd][hf] = *(LAS const bf16x8*)(lds + L2_KV + wc * 6144 + (jd * 2 + hf) * 1024 + lane * 16);
    ST16(ofb, o_block(kf, ph[0][0], ph[0][1]));
    ST16(ofb + 1024, o_block(kf, ph[1][0], ph[1][1]));
    SCHED();
    WAITV(18); BAR(); tile(19);
    ST16(ofb + 2048, o_block(kf, ph[2][0], ph[2][1]));
    ST16(ofb + 3072, o_block(kf, ph[3][0], ph[3][1]));
    SCHED();
    WAITV(20); BAR(); tile(20);
    WAITV(20); BAR(); tile(21);
    WAITV(18); BAR(); tile(22);
    t = 19;
  }
  for (; t < NT2; ++t) {
    if (t + D2 - 1 < NT2) WAITV(16); else WAITV(0);
    BAR();
    if (t + D2 < NT2) tile(t + D2);
  }
  asm volatile(".Lk_main_late:");
}

DI void consumer2(LAS char* lds, const Args& a, const int type, const int p, const int j, const unsigned tag, u32x2 (&xr)[4][2], bf16x8 (&ph)[4][2]) {
  char* ws = a.ws;
  int tz = threadIdx.x;
  asm volatile("" : "+v"(tz));
  const int lane = tz & 63, cw = __builtin_amdgcn_readfirstlane(tz >> 6) & 3;
  const int bid = blockIdx.x, xcd = bid & 7, idx = bid >> 3, xq = xcd >> 1;
  const int kh = xcd & 1, rt = 8 * (xq >> 1) + (idx & 7), ct = 4 * (xq & 1) + (idx >> 3);
  const int fr = lane & 15, fq = lane >> 4, q4 = fr >> 2, p4 = lane & 3;
  gu32* cnt = (gu32*)(ws + WS_CNT);
  const int wm = cw >> 1, wn = cw & 1;
  f32x4 acc[4][4];
#pragma unroll
  for (int n = 0; n < 4; ++n)
#pragma unroll
    for (int e = 0; e < 2; ++e) {
      acc[n][e] = (f32x4){bf_lo(xr[n][e].x), bf_hi(xr[n][e].x), bf_lo(xr[n][e].y), bf_hi(xr[n][e].y)};
      acc[n][2 + e] = (f32x4){0.f, 0.f, 0.f, 0.f};
    }
  LAS const char* pA = lds + wm * 4096 + lane * 16;
  const int kr = 4 * fq + q4;
  const int sw = (kr >> 1) & 3;
  LAS const char* pB[4];
#pragma unroll
  for (int n = 0; n < 4; ++n) pB[n] = lds + 16384 + wn * 8192 + kr * 128 + 32 * (n ^ sw) + 8 * p4;
  asm volatile("" : "+v"(pA), "+v"(pB[0]), "+v"(pB[1]), "+v"(pB[2]), "+v"(pB[3]));
  auto flip = [&]() {
    pA = (LAS const char*)(size_t)((unsigned)(size_t)pA ^ 65536u);
#pragma unroll
    for (int n = 0; n < 4; ++n) pB[n] = (LAS const char*)(size_t)((unsigned)(size_t)pB[n] ^ 65536u);
    asm volatile("" : "+v"(pA), "+v"(pB[0]), "+v"(pB[1]), "+v"(pB[2]), "+v"(pB[3]));
  };
#pragma unroll
  for (int n = 0; n < 4; ++n) asm volatile("" : "+v"(acc[n][0]), "+v"(acc[n][1]), "+v"(acc[n][2]), "+v"(acc[n][3]));
  bf16x8 A0[4], A1[4];
  s16x4 B0[4][2], B1[4][2];
  auto rd1 = [&](bf16x8 (&A)[4], s16x4 (&B)[4][2], int slot, int ks, int k) {
    const int so = slot * ST2;
    LAS const char* pa = pA;
    const int ai = k == 0 ? 0 : (k >= 3 && k <= 5) ? k - 2 : -1;
    if (ai >= 0) A[ai] = *(LAS const bf16x8*)(pa + so + ks * 8192 + ai * 1024);
    else {
      const int kb = k < 3 ? k - 1 : k - 4;
      const int n = kb >> 1, h = kb & 1;
      LAS const char* pb = pB[n];
      B[n][h] = __builtin_amdgcn_ds_read_tr16_b64_v4i16((LAS s16x4*)(pb + so + ks * 4096 + h * 2048));
    }
  };
  auto step = [&](bf16x8 (&A)[4], s16x4 (&B)[4][2], bf16x8 (&An)[4], s16x4 (&Bn)[4][2], int nslot, int nks) {
#pragma unroll
    for (int n = 0; n < 4; ++n) {
      const bf16x8 bf = __builtin_bit_cast(bf16x8, __builtin_shufflevector(B[n][0], B[n][1], 0, 1, 2, 3, 4, 5, 6, 7));
#pragma unroll
      for (int i = 0; i < 4; ++i) {
        mfma16m(acc[n][i], bf, A[i]);
        if (n * 4 + i < 12) rd1(An, Bn, nslot, nks, n * 4 + i);
      }
    }
  };
  gu32* sig = cnt + 64 * ((type ? CNT_O : CNT_X) + p * 2 + (j >> 3)) + (j & 7);
  gu32* wx = type ? cnt + 64 * (CNT_KV + j) + (lane & 31) : cnt + 64 * (CNT_X + (rt >> 1) * 2 + kh) + (lane & 7);
  const unsigned evC = (type ? 0x1050u : 0x1040u);
  const unsigned evW = type ? 0x400u : 0x08u;
  const unsigned evA = (cw == 0 ? evC : 0u) | evW | (type ? 0x100u : 0u);
  const unsigned evB = (cw == 0 ? ((evC >> 1) | evW) : 0u) | (type ? 0u : 1u);
  const auto rws = __builtin_amdgcn_make_buffer_rsrc(ws, 0, WS_USED, 0x00020000);
  unsigned pollv = 0;
  BAR();
#pragma unroll
  for (int k = 0; k < 12; ++k) rd1(A0, B0, 0, 0, k);
  for (int e = 0; e < NT2 / 2; ++e) {
    step(A0, B0, A1, B1, 0, 1);
    if ((evA >> e) & 1) {
      asm volatile("s_waitcnt vmcnt(0)" : "+v"(pollv)::"memory");
      if (cw == 0 && ((evC >> e) & 1)) {
        if (!__all(pollv == tag)) wait_tag(wx, tag);
        asm volatile("" : "+v"(tz));
        wx = cnt + 64 * ((e < 6 ? CNT_X : CNT_O) + (rt >> 1) * 2 + kh) + (tz & 7);
        if ((tz & 8) && e == 6) wx = cnt + 64 * CNT_IN + (bid ^ 1);
      }
      if (type && e == 8) {
        asm volatile("" : "+v"(tz));
        const int l2 = tz & 63;
        bf16x8 kf[3][2];
#pragma unroll
        for (int jd = 0; jd < 3; ++jd)
#pragma unroll
          for (int hf = 0; hf < 2; ++hf) kf[jd][hf] = *(LAS const bf16x8*)(lds + L2_KV + wn * 6144 + (jd * 2 + hf) * 1024 + l2 * 16);
        const int rbw = 16 * p + 8 + 4 * wm;
        const unsigned ofb = WS_AF + ((unsigned)(rbw >> 3) * 144 * 8 + (rbw & 7)) * 1024 + l2 * 16 + (112 + 2 * j + wn) * 8192;
#pragma unroll
        for (int i = 0; i < 4; ++i) ST16(ofb + i * 1024, o_block(kf, ph[i][0], ph[i][1]));
      }
    }
    WAITL0();
    BAR();
    if ((evB >> e) & 1) {
      asm volatile("" : "+v"(tz));
      const unsigned l2 = tz & 63;
      if (e == 0) {
        const int rbw = 16 * p + 8 + 4 * wm;
        const unsigned o0 = WS_AF + ((unsigned)(rbw >> 3) * 144 * 8 + (rbw & 7)) * 1024 + l2 * 16 + (64 + 3 * j + wn) * 8192;
#pragma unroll
        for (int i = 0; i < 4; ++i) ST16(o0 + i * 1024, ph[i][0]);
      }
      if (((evW >> e) & 1) && tz < 258) __hip_atomic_store((tz & 1) ? cnt + 64 * CNT_IN + bid : sig, tag, RLX_AGENT);
      if (cw == 0 && ((evC >> (e + 1)) & 1)) asm volatile("global_load_dword %0, %1, off sc1" : "=v"(pollv) : "v"(wx) : "memory");
    }
    step(A1, B1, A0, B0, 1, 0);
    step(A0, B0, A1, B1, 1, 1);
    WAITL0();
    BAR();
    flip();
    step(A1, B1, A0, B0, 0, 0);
  }
  asm volatile("s_nop 15" ::: "memory");
  SCHED();
  asm volatile("" : "+v"(tz));
  const int ln = tz & 63;
  const auto rx = __builtin_amdgcn_make_buffer_rsrc(ws + WS_XCH, 0, 256u * 16384u, 0x00020000);
  const unsigned xo_send = (unsigned)bid * 16384u + cw * 4096 + ln * 16, xo_recv = (unsigned)(bid ^ 1) * 16384u + cw * 4096 + ln * 16;
#pragma unroll
  for (int e = 0; e < 2; ++e)
#pragma unroll
    for (int n = 0; n < 4; n += 2) {
      u32x4 v = __builtin_bit_cast(u32x4, pack8(acc[n][2 + e], acc[n + 1][2 + e]));
#pragma unroll
      for (int r = 0; r < 4; ++r) v[r] = v[r] < 0xfffffffeu ? v[r] : 0xfffffffeu;
      __builtin_amdgcn_raw_buffer_store_b128(v, rx, xo_send + (e * 2 + (n >> 1)) * 1024, 0, 16);
    }
  u32x4 pv[4];
#pragma unroll 1
  for (unsigned spins = 0; spins < (1u << 20); ++spins) {
#pragma unroll
    for (int k = 0; k < 4; ++k) pv[k] = __builtin_amdgcn_raw_buffer_load_b128(rx, xo_recv + k * 1024, 0, 16);
    unsigned m = 0;
#pragma unroll
    for (int k = 0; k < 4; ++k) m = max(max(m, max(pv[k].x, pv[k].y)), max(pv[k].z, pv[k].w));
    if (!__any(m == 0xffffffffu)) break;
    asm volatile("" ::: "memory");
    __builtin_amdgcn_s_sleep(2);
  }
  const auto rout = __builtin_amdgcn_make_buffer_rsrc(a.out, 0, S_ * D_ * 2, 0x00020000);
  const int rowz = 128 * rt + 64 * wm + 32 * kh + (ln & 15);
  const int colw = 128 * ct + 64 * wn + 16 * ((ln >> 4) & 1) + 8 * (ln >> 5);
#pragma unroll
  for (int e = 0; e < 2; ++e) {
    u32x4 w[2];
#pragma unroll
    for (int n = 0; n < 4; n += 2) {
      const u32x4 v = pv[e * 2 + (n >> 1)];
      const f32x4 s = acc[n][e], t = acc[n + 1][e];
      const unsigned ax = cvtpk(s[0] + bf_lo(v.x), s[1] + bf_hi(v.x)), ay = cvtpk(s[2] + bf_lo(v.y), s[3] + bf_hi(v.y));
      const unsigned bx = cvtpk(t[0] + bf_lo(v.z), t[1] + bf_hi(v.z)), by = cvtpk(t[2] + bf_lo(v.w), t[3] + bf_hi(v.w));
      const auto sx = __builtin_amdgcn_permlane16_swap(ax, bx, false, false);
      const auto sy = __builtin_amdgcn_permlane16_swap(ay, by, false, false);
      w[n >> 1] = (u32x4){sx[0], sy[0], sx[1], sy[1]};
    }
    __builtin_amdgcn_raw_buffer_store_b128(w[0], rout, (unsigned)((rowz + 16 * e) * D_ + colw) * 2u, 0, 16);
    __builtin_amdgcn_raw_buffer_store_b128(w[1], rout, (unsigned)((rowz + 16 * e) * D_ + colw + 32) * 2u, 0, 16);
  }
  if (bid == 0 && tz == 256) __hip_atomic_store(cnt + 64 * CNT_EPOCH, tag, RLX_AGENT);
}

__global__ __launch_bounds__(512, 2) void k_main(Args a) {
  extern __shared__ __attribute__((aligned(16))) char smem[];
  LAS char* lds = (LAS char*)smem;
  const int bid = blockIdx.x, xcd = bid & 7, idx = bid >> 3;
  const int p = idx & 7, c = 4 * xcd + (idx >> 3);
  const int G = __builtin_amdgcn_readfirstlane(threadIdx.x >> 8);

  u32x2 xr[4][2];
  bf16x8 ph[4][2];
  const unsigned tag = phase0(lds, a, p, c);
  if (G == 0) {
    if (c & 1) phase1<1, 0>(lds, a, p, c >> 1, tag, xr, ph); else phase1<0, 0>(lds, a, p, c >> 1, tag, xr, ph);
    producer2(lds, a, c & 1, p, c >> 1, ph);
  } else {
    if (c & 1) phase1<1, 1>(lds, a, p, c >> 1, tag, xr, ph); else phase1<0, 1>(lds, a, p, c >> 1, tag, xr, ph);
    consumer2(lds, a, c & 1, p, c >> 1, tag, xr, ph);
  }
}

extern "C" void kernel_launch(void* const* d_in, const int* in_sizes, int n_in, void* d_out, int out_size, void* d_ws,
                              size_t ws_size, hipStream_t stream) {
  Args a;
  a.x = (const u16*)d_in[0];
  a.wqkv = (const u16*)d_in[1];
  a.wphi = (const u16*)d_in[2];
  a.wo = (const u16*)d_in[3];
  a.wgate = (const u16*)d_in[5];
  a.wup = (const u16*)d_in[6];
  a.wdown = (const u16*)d_in[7];
  a.ws = (char*)d_ws;
  a.out = (u16*)d_out;
  (void)hipFuncSetAttribute((const void*)k_main, hipFuncAttributeMaxDynamicSharedMemorySize, L_TOTAL);
  k_main<<<256, 512, L_TOTAL, stream>>>(a);
}
```
